```python
import torch

def _softmax(x, axis=-1):
    return torch.softmax(x, dim=axis)
def _gelu(x):
    return torch.nn.functional.gelu(x, approximate="tanh")
def _silu(x):
    return torch.nn.functional.silu(x)
def _sigmoid(x):
    return torch.sigmoid(x)
def _layernorm(x, axis=-1, eps=1e-5):
    m = x.mean(dim=axis, keepdim=True)
    v = x.var(dim=axis, keepdim=True, unbiased=False)
    return (x - m) / torch.sqrt(v + eps)
def _scatter_add(tgt, idx, src, axis=0):
    return tgt.clone().index_add_(axis, idx, src)
def _segment_sum(src, seg, n):
    out = torch.zeros((n,) + src.shape[1:], dtype=src.dtype)
    return out.index_add_(0, seg, src)
def _pad(x, pw):
    flat = tuple(v for pair in reversed(pw) for v in pair)
    return torch.nn.functional.pad(x, flat)

kernel_name = 'standalone__gqa_block_cached__decode'

def setup_inputs():

    _w = (lambda a: (lambda t: t.to(torch.bfloat16) if t.is_floating_point() else t)(torch.as_tensor(a)))
    B, Sq, D = 8, 1, 640
    Hq, Hkv, Sk, Dh = 32, 32, 4096, 192
    assert Hq % Hkv == 0, (Hq, Hkv)
    return {
        "x": _w((lambda *s: torch.randn(*s, dtype=torch.bfloat16) * 0.8)(B, Sq, D)),
        "k_cache": _w((lambda *s: torch.randn(*s, dtype=torch.bfloat16) * 0.8)(B, Hkv, Sk, Dh)),
        "v_cache": _w((lambda *s: torch.randn(*s, dtype=torch.bfloat16) * 0.05)(B, Hkv, Sk, Dh)),
        "wq": _w((lambda *s: torch.randn(*s, dtype=torch.bfloat16) * 0.8)(D, Hq * Dh) * (D ** -0.5)),
        "wo": _w((lambda *s: torch.randn(*s, dtype=torch.bfloat16) * 0.05)(Hq * Dh, D) * ((Hq * Dh) ** -0.5)),
    }

def reference(x, k_cache, v_cache, wq, wo):
    B, Sq, D = x.shape
    Hq, Dh = 32, 192
    Hkv = k_cache.shape[1]
    rep = Hq // Hkv
    q = (lambda x, *d: x.permute(*d))((x @ wq).reshape(B, Sq, Hq, Dh), 0, 2, 1, 3)
    k = (lambda x, n, axis: torch.repeat_interleave(x, n, dim=axis))(k_cache, rep, axis=1)
    v = (lambda x, n, axis: torch.repeat_interleave(x, n, dim=axis))(v_cache, rep, axis=1)
    scores = torch.einsum("bhqd,bhkd->bhqk", q, k) * (Dh ** -0.5)
    p = _softmax(scores, axis=-1)
    o = torch.einsum("bhqk,bhkd->bhqd", p, v)
    return (lambda x, *d: x.permute(*d))(o, 0, 2, 1, 3).reshape(B, Sq, Hq * Dh) @ wo
```

```cpp
#include <hip/hip_runtime.h>
#include <hip/hip_bf16.h>
#include <stdint.h>

using u32x4 = __attribute__((ext_vector_type(4))) unsigned;
using u32x2 = __attribute__((ext_vector_type(2))) unsigned;
using u32x3 = __attribute__((ext_vector_type(3))) unsigned;
using f32x4 = __attribute__((ext_vector_type(4))) float;
using bf16x2 = __attribute__((ext_vector_type(2))) __bf16;

constexpr int NH = 32, SK = 4096, DH = 192, DM = 640;
constexpr int NTHR = 512;
constexpr int NG = SK / 8 / 8;
constexpr size_t GB = 3072;
constexpr size_t GSTRIDE = 8 * GB;
constexpr float C2 = 0.07216878364870322f * 1.4426950408889634f;
constexpr int NPRE = 3;
constexpr int PD = 3;
constexpr int WB = 48;
constexpr int DLY = 5, PSLOW = 24, PFAST = 1;
constexpr size_t WS_EPOCH = 0;
constexpr size_t WS_QG = 4096;
constexpr size_t WS_GRAN = WS_QG + 256 * 768;
constexpr unsigned TAG_STEP = 0x9E3779B9u;
constexpr unsigned SPIN_CAP = 1u << 22;

__device__ __forceinline__ float dot2(unsigned a, unsigned b, float c) {
  return __builtin_amdgcn_fdot2_f32_bf16(__builtin_bit_cast(bf16x2, a), __builtin_bit_cast(bf16x2, b), c, false);
}
template <int CTRL> __device__ __forceinline__ float dpp(float x) {
  return __builtin_bit_cast(float, __builtin_amdgcn_mov_dpp(__builtin_bit_cast(int, x), CTRL, 0xf, 0xf, true));
}
constexpr int XOR1 = 0xB1, XOR2 = 0x4E, XOR7 = 0x141, XOR8 = 0x128;
__device__ __forceinline__ float swap32_add(float a, float b) {
  auto t = __builtin_amdgcn_permlane32_swap(__float_as_uint(a), __float_as_uint(b), false, false);
  return __uint_as_float(t[0]) + __uint_as_float(t[1]);
}
__device__ __forceinline__ float swap16_add(float a, float b) {
  auto t = __builtin_amdgcn_permlane16_swap(__float_as_uint(a), __float_as_uint(b), false, false);
  return __uint_as_float(t[0]) + __uint_as_float(t[1]);
}
__device__ __forceinline__ void treduce24(const float (&v)[24], float (&o)[3], bool b3) {
#pragma unroll
  for (int j = 0; j < 3; ++j) {
    float a[4], b[2];
#pragma unroll
    for (int i = 0; i < 4; ++i) a[i] = swap32_add(v[j * 8 + i], v[j * 8 + i + 4]);
#pragma unroll
    for (int i = 0; i < 2; ++i) b[i] = swap16_add(a[i], a[i + 2]);
    const float keep = b3 ? b[1] : b[0], give = b3 ? b[0] : b[1];
    o[j] = keep + dpp<XOR8>(give);
  }
}
__device__ __forceinline__ float xor4(float x) {
  int t = __builtin_amdgcn_update_dpp(0, __builtin_bit_cast(int, x), 0x104, 0xf, 0x5, false);
  t = __builtin_amdgcn_update_dpp(t, __builtin_bit_cast(int, x), 0x114, 0xf, 0xa, false);
  return __builtin_bit_cast(float, t);
}
__device__ __forceinline__ float rsum(float x) {
  x += dpp<XOR8>(x); x = swap16_add(x, x); return swap32_add(x, x);
}
__device__ __forceinline__ float rmax(float x) {
  x = fmaxf(x, dpp<XOR8>(x));
  auto s = __builtin_amdgcn_permlane16_swap(__float_as_uint(x), __float_as_uint(x), false, false);
  x = fmaxf(__uint_as_float(s[0]), __uint_as_float(s[1]));
  auto t = __builtin_amdgcn_permlane32_swap(__float_as_uint(x), __float_as_uint(x), false, false);
  return fmaxf(__uint_as_float(t[0]), __uint_as_float(t[1]));
}
__device__ __forceinline__ float sum32(float x) {
  x += dpp<XOR1>(x); x += dpp<XOR2>(x); x += dpp<XOR7>(x); x += dpp<XOR8>(x);
  return swap16_add(x, x);
}
template <int I> __device__ __forceinline__ float bcast8(float x) {
  int t = __builtin_amdgcn_update_dpp(0, __builtin_bit_cast(int, x), 0x150 + I, 0xf, 0x3, false);
  t = __builtin_amdgcn_update_dpp(t, __builtin_bit_cast(int, x), 0x150 + I + 8, 0xf, 0xc, false);
  return __builtin_bit_cast(float, t);
}
__device__ __forceinline__ unsigned cvtpk(float lo, float hi) {
  unsigned r; asm("v_cvt_pk_bf16_f32 %0, %1, %2" : "=v"(r) : "v"(lo), "v"(hi)); return r;
}
__device__ __forceinline__ float bflo(unsigned x) { return __uint_as_float(x << 16); }
__device__ __forceinline__ float bfhi(unsigned x) { return __uint_as_float(x & 0xffff0000u); }

#define GLD4_NT(dst, voff, sbase, imm) asm volatile("global_load_dwordx4 %0, %1, %2 offset:%3 nt" : "+v"(dst) : "v"(voff), "s"(sbase), "i"(imm))
#define GLD4(dst, voff, sbase, imm) asm volatile("global_load_dwordx4 %0, %1, %2 offset:%3" : "=v"(dst) : "v"(voff), "s"(sbase), "i"(imm))
#define GLD3(dst, voff, sbase) asm volatile("global_load_dwordx3 %0, %1, %2" : "=v"(dst) : "v"(voff), "s"(sbase))
#define GLD1(dst, voff, sbase, imm) asm volatile("global_load_dword %0, %1, %2 offset:%3" : "=v"(dst) : "v"(voff), "s"(sbase), "i"(imm))
#define GLD1_SC1(dst, voff, sbase) asm volatile("global_load_dword %0, %1, %2 sc1" : "=v"(dst) : "v"(voff), "s"(sbase))
#define GLD4_SC1(dst, voff, sbase, imm) asm volatile("global_load_dwordx4 %0, %1, %2 offset:%3 sc1" : "=v"(dst) : "v"(voff), "s"(sbase), "i"(imm) : "memory")
#define WAIT3(n, a, b, c) asm volatile("s_waitcnt vmcnt(%3)" : "+v"(a), "+v"(b), "+v"(c) : "i"(n))
#define GST4_SC1(voff, sbase, data, imm) asm volatile("global_store_dwordx4 %0, %1, %2 offset:%3 sc1\n\ts_nop 1" ::"v"(voff), "v"(data), "s"(sbase), "i"(imm) : "memory")
#define GST2_SC1(voff, sbase, data) asm volatile("global_store_dwordx2 %0, %1, %2 sc1\n\ts_nop 1" ::"v"(voff), "v"(data), "s"(sbase) : "memory")

constexpr int L_PART = 0;
constexpr int L_Q = 8 * 200;
constexpr int L_RED = L_Q + 128;
constexpr int L_END = L_RED + 6 * 640;

__global__ void __launch_bounds__(NTHR, 2)
attn_fused(const char* __restrict__ x, const char* __restrict__ K, const char* __restrict__ V,
           const char* __restrict__ wq, const char* __restrict__ wo, unsigned* __restrict__ out, char* __restrict__ ws) {
  __shared__ __attribute__((aligned(16))) float lds[L_END];
  const int tid = threadIdx.x, lane = tid & 63, wave = __builtin_amdgcn_readfirstlane(tid >> 6);
  const int blk = blockIdx.x;
  const int h = (blk & 7) * 4 + ((blk >> 3) & 3), b = blk >> 5;
  const int r = lane >> 3, c = lane & 7;

  unsigned ep;
  GLD1_SC1(ep, 0u, ws + WS_EPOCH);
  const int cg = lane & 3, rho = lane >> 2;
  const int kb = rho < 8 ? 6 * rho : 4 * rho + 14;
  u32x3 XR[8], WR[6];
#pragma unroll
  for (int i = 0; i < 6; ++i) GLD3(WR[i], kb * (NH * DH * 2) + cg * 12, wq + ((size_t)(wave * 80 + i) * (NH * DH) + h * DH + b * 24) * 2);
#pragma unroll
  for (int i = 0; i < 8; ++i) GLD3(XR[i], kb * 2, x + (size_t)(i * DM + wave * 80) * 2);
  const size_t hb = (size_t)(b * NH + h) * SK * DH * 2;
  const char* kp = K + hb + (size_t)wave * GB;
  const char* vp = V + hb + (size_t)wave * GB;
  const unsigned lo = r * 384 + c * 16;
  const int rot = (33 * ((blk >> 3) & 3) + 12 * (blk & 7) + 18 * (blk >> 5)) & (NG - 1);
#define GI(g) (((g) + rot) & (NG - 1))
#define GOFF(g) ((size_t)GI(g) * GSTRIDE)
  constexpr int KR = 16, DK = 4, RING = 4;
  u32x4 R[KR][3];
#define ISSUE3(X, ptr) { const char* p_ = (ptr); GLD4_NT(X[0], lo, p_, 0); GLD4_NT(X[1], lo, p_, 128); GLD4_NT(X[2], lo, p_, 256); }
#define FRESH3(X) asm volatile("" : "=v"(X[0]), "=v"(X[1]), "=v"(X[2]))
  constexpr int QW = 7;
#define TIE_Q "+v"(ep), "+v"(WR[0]), "+v"(WR[1]), "+v"(WR[2]), "+v"(WR[3]), "+v"(WR[4]), "+v"(WR[5]), \
              "+v"(XR[0]), "+v"(XR[1]), "+v"(XR[2]), "+v"(XR[3]), "+v"(XR[4]), "+v"(XR[5]), "+v"(XR[6]), "+v"(XR[7])
#pragma unroll
  for (int s = 0; s < NPRE; ++s) { FRESH3(R[s]); ISSUE3(R[s], kp + GOFF(s)); }
  asm volatile("s_waitcnt vmcnt(%15)" : TIE_Q : "i"(3 * NPRE));
  const unsigned tag = ep + TAG_STEP;

  float acc[24];
  {
    float a[48];
#pragma unroll
    for (int v = 0; v < 48; ++v) a[v] = 0.f;
#pragma unroll
    for (int m = 0; m < 3; ++m) {
      unsigned wp[6];
#pragma unroll
      for (int d = 0; d < 3; ++d) {
        wp[2 * d] = __builtin_amdgcn_perm(WR[2 * m + 1][d], WR[2 * m][d], 0x05040100u);
        wp[2 * d + 1] = __builtin_amdgcn_perm(WR[2 * m + 1][d], WR[2 * m][d], 0x07060302u);
      }
#pragma unroll
      for (int bb = 0; bb < 8; ++bb) {
        const unsigned xv = (m == 0 && rho >= 8) ? 0u : XR[bb][m];
#pragma unroll
        for (int cc_ = 0; cc_ < 6; ++cc_) a[bb * 6 + cc_] = dot2(wp[cc_], xv, a[bb * 6 + cc_]);
      }
    }
    float a1[24], a2[12], a3[6], a4[3];
#pragma unroll
    for (int v = 0; v < 24; ++v) a1[v] = swap32_add(a[v], a[v + 24]);
#pragma unroll
    for (int v = 0; v < 12; ++v) a2[v] = swap16_add(a1[v], a1[v + 12]);
    const bool b3 = (lane & 8) != 0, b2 = (lane & 4) != 0;
#pragma unroll
    for (int v = 0; v < 6; ++v) { const float keep = b3 ? a2[v + 6] : a2[v], give = b3 ? a2[v] : a2[v + 6]; a3[v] = keep + dpp<XOR8>(give); }
#pragma unroll
    for (int v = 0; v < 3; ++v) { const float keep = b2 ? a3[v + 3] : a3[v], give = b2 ? a3[v] : a3[v + 3]; a4[v] = keep + xor4(give); }
#pragma unroll
    for (int v = 0; v < 3; ++v) lds[L_PART + wave * 200 + (lane >> 3) * 24 + cg * 6 + (b2 ? 3 : 0) + v] = a4[v];
  }
  __syncthreads();
  if (wave == QW) {
    const char* qg = ws + WS_QG + (size_t)h * (8 * 768);
    const int ql = lane < 48 ? lane : 47;
    {
      f32x4 s4 = *(const f32x4*)&lds[L_PART + ql * 4];
#pragma unroll
      for (int w = 1; w < 8; ++w) s4 += *(const f32x4*)&lds[L_PART + w * 200 + ql * 4];
      const u32x4 g4 = {cvtpk(s4[0], s4[1]), tag, cvtpk(s4[2], s4[3]), tag};
      GST4_SC1((unsigned)(b * 768 + ql * 16), qg, g4, 0);
    }
  }
#pragma unroll
  for (int s = NPRE; s < KR; ++s) {
    if (wave != QW) asm volatile("s_waitcnt vmcnt(%0)" :: "i"(3 * (PD - 1)));
    FRESH3(R[s]); ISSUE3(R[s], kp + GOFF(s));
  }
  if (wave == QW) {
    const char* qg = ws + WS_QG + (size_t)h * (8 * 768);
    const int ql = lane < 48 ? lane : 47;
    u32x4 g;
    const unsigned qo = (ql / 6) * 768 + b * 96 + (ql % 6) * 16;
    for (unsigned spins = 0; spins < SPIN_CAP; ++spins) {
      asm volatile("global_load_dwordx4 %0, %1, %2 sc1\n\ts_waitcnt vmcnt(0)" : "=v"(g) : "v"(qo), "s"(qg) : "memory");
      if (__all(g[1] == tag && g[3] == tag)) break;
      __builtin_amdgcn_s_sleep(1);
    }
    *(u32x2*)&((unsigned*)lds)[L_Q + (ql / 6) * 12 + (ql % 6) * 2] = u32x2{g[0], g[2]};
  }
  __syncthreads();
  u32x4 q[3];
#pragma unroll
  for (int j = 0; j < 3; ++j) q[j] = *(const u32x4*)&((unsigned*)lds)[L_Q + j * 32 + c * 4];
  asm volatile("" : "+v"(q[0]), "+v"(q[1]), "+v"(q[2]));

  if ((blk & 1) == 0) {
#pragma unroll 1
    for (int z = 0; z < DLY; ++z) __builtin_amdgcn_s_sleep(16);
  }
  float S[8];
#pragma unroll
  for (int j = 0; j < 8; ++j) S[j] = 0.f;
#define KSTEP(i, NW, PRE, POST, NEXT) { \
    WAIT3(NW, R[i][0], R[i][1], R[i][2]); \
    if (PRE) ISSUE3(R[((i) + DK) % KR], NEXT); \
    asm volatile("" : "+v"(R[i][0]), "+v"(R[i][1]), "+v"(R[i][2]));     \
    float d0 = 0.f, d1 = 0.f, d2 = 0.f; \
    _Pragma("unroll") for (int e = 0; e < 4; ++e) { d0 = dot2(R[i][0][e], q[0][e], d0); d1 = dot2(R[i][1][e], q[1][e], d1); d2 = dot2(R[i][2][e], q[2][e], d2); } \
    asm volatile("" : "+v"(d0), "+v"(d1), "+v"(d2)); \
    if (POST) ISSUE3(R[i], kp + GOFF(KR + (i))); \
    float sc = (d0 + d1) + d2; \
    sc += dpp<XOR1>(sc); sc += dpp<XOR2>(sc); sc += dpp<XOR7>(sc); \
    cur = c == ((i) & 7) ? sc : cur; \
    if (((i) & 7) == 7) { _Pragma("unroll") for (int k = 0; k < 7; ++k) S[k] = S[k + 1]; S[7] = cur; } }
#define KITEM(i, FIRST) KSTEP(i, (FIRST) ? 3 * (KR - 1 - (i) + ((i) < DK ? (i) : DK)) : 3 * (DK - 1), !(FIRST), (FIRST) && (i) < DK, \
                              ((i) + DK < KR ? kp : hi) + GOFF(KR * j + (i) + DK))
#define KBLOCK(FIRST) { \
    const char* hi = j == 3 ? vp : kp; \
    float cur = 0.f; \
    KITEM(0, FIRST); KITEM(1, FIRST); KITEM(2, FIRST); KITEM(3, FIRST); KITEM(4, FIRST); KITEM(5, FIRST); KITEM(6, FIRST); KITEM(7, FIRST); \
    KITEM(8, FIRST); KITEM(9, FIRST); KITEM(10, FIRST); KITEM(11, FIRST); KITEM(12, FIRST); KITEM(13, FIRST); KITEM(14, FIRST); KITEM(15, FIRST); }
  { const int j = 0; KBLOCK(true); }
#pragma unroll 1
  for (int j = 1; j < 4; ++j) KBLOCK(false);
  float M, l;
  {
#pragma unroll
    for (int j = 0; j < 8; ++j) S[j] *= C2;
    M = fmaxf(fmaxf(fmaxf(S[0], S[1]), fmaxf(S[2], S[3])), fmaxf(fmaxf(S[4], S[5]), fmaxf(S[6], S[7])));
    M = fmaxf(M, dpp<XOR1>(M)); M = fmaxf(M, dpp<XOR2>(M)); M = fmaxf(M, dpp<XOR7>(M));
    M = rmax(M);
#pragma unroll
    for (int j = 0; j < 8; ++j) S[j] = __builtin_amdgcn_exp2f(S[j] - M);
    l = ((S[0] + S[1]) + (S[2] + S[3])) + ((S[4] + S[5]) + (S[6] + S[7]));
    l += dpp<XOR1>(l); l += dpp<XOR2>(l); l += dpp<XOR7>(l);
    l = rsum(l);
  }
  __builtin_amdgcn_s_barrier();

#pragma unroll
  for (int e = 0; e < 24; ++e) acc[e] = 0.f;
#define PIN_ACC() asm volatile("" : "+v"(acc[0]), "+v"(acc[1]), "+v"(acc[2]), "+v"(acc[3]), "+v"(acc[4]), "+v"(acc[5]), "+v"(acc[6]), "+v"(acc[7]), "+v"(acc[8]), "+v"(acc[9]), "+v"(acc[10]), "+v"(acc[11]), \
    "+v"(acc[12]), "+v"(acc[13]), "+v"(acc[14]), "+v"(acc[15]), "+v"(acc[16]), "+v"(acc[17]), "+v"(acc[18]), "+v"(acc[19]), "+v"(acc[20]), "+v"(acc[21]), "+v"(acc[22]), "+v"(acc[23]))
#define VSTEP(s, i, REISSUE, NEXT, NW) { \
    const float p = bcast8<i>(S[0]); \
    WAIT3(NW, R[s][0], R[s][1], R[s][2]); \
    _Pragma("unroll") for (int j_ = 0; j_ < 3; ++j_) _Pragma("unroll") for (int e = 0; e < 4; ++e) { \
      acc[j_ * 8 + 2 * e] = fmaf(p, bflo(R[s][j_][e]), acc[j_ * 8 + 2 * e]); \
      acc[j_ * 8 + 2 * e + 1] = fmaf(p, bfhi(R[s][j_][e]), acc[j_ * 8 + 2 * e + 1]); } \
    PIN_ACC(); \
    if (REISSUE) ISSUE3(R[s], NEXT); }
#define SHIFT_S() _Pragma("unroll") for (int k = 0; k < 7; ++k) S[k] = S[k + 1]
#define VLOOP(i) VSTEP((i) % RING, i, true, vp + GOFF(8 * j + (i) + RING), 3 * (RING - 1))
#pragma unroll 1
  for (int j = 0; j < 6; ++j) { VLOOP(0); VLOOP(1); VLOOP(2); VLOOP(3); VLOOP(4); VLOOP(5); VLOOP(6); VLOOP(7); SHIFT_S(); }

  const int cc = tid % 80, rg = tid / 80;
  u32x4 WO[32];
  const char* wob = wo + (size_t)(h * DH) * DM * 2;
  const unsigned wof = ((rg < 6 ? rg : 5) * 32 * DM + cc * 8) * 2;
#define WO_BURST() _Pragma("unroll") for (int i_ = 0; i_ < 32; ++i_) { const char* wr = wob + (size_t)(i_ >> 1) * (2 * DM * 2); GLD4(WO[i_], wof, wr, (i_ & 1) * DM * 2); }
#define WO_PAIRUP() _Pragma("unroll") for (int i_ = 0; i_ < 32; i_ += 2) { \
    asm volatile("" : "+v"(WO[i_]), "+v"(WO[i_ + 1])); \
    _Pragma("unroll") for (int e = 0; e < 4; ++e) { \
      const unsigned a_ = WO[i_][e], b_ = WO[i_ + 1][e]; \
      WO[i_][e] = __builtin_amdgcn_perm(b_, a_, 0x05040100u); \
      WO[i_ + 1][e] = __builtin_amdgcn_perm(b_, a_, 0x07060302u); } \
    asm volatile("" : "+v"(WO[i_]), "+v"(WO[i_ + 1])); }
#define VLAST(T) { if ((T) == WB) { WO_BURST(); } \
    VSTEP((T) % RING, (T) & 7, (T) + RING < NG, vp + GOFF((T) + RING), 3 * ((T) + RING <= NG ? RING - 1 : NG - 1 - (T)) + ((T) >= WB && (T) < WB + RING ? 32 : 0)); \
    if ((T) == WB + RING) { WO_PAIRUP(); } \
    if (((T) & 7) == 7) { SHIFT_S(); } }
  VLAST(48); VLAST(49); VLAST(50); VLAST(51); VLAST(52); VLAST(53); VLAST(54); VLAST(55);
  VLAST(56); VLAST(57); VLAST(58); VLAST(59); VLAST(60); VLAST(61); VLAST(62); VLAST(63);

  {
    float o3[3];
    treduce24(acc, o3, (lane & 8) != 0);
#pragma unroll
    for (int j = 0; j < 3; ++j) lds[L_PART + wave * 200 + j * 64 + c * 8 + r] = o3[j];
    if (lane == 0) *(float2*)&lds[L_PART + wave * 200 + 192] = make_float2(M, l);
  }
  __syncthreads();
  if (tid < 96) {
    float2 ml[8], pv[8];
#pragma unroll
    for (int w = 0; w < 8; ++w) { ml[w] = *(const float2*)&lds[L_PART + w * 200 + 192]; pv[w] = *(const float2*)&lds[L_PART + w * 200 + 2 * tid]; }
    float Mx = ml[0].x;
#pragma unroll
    for (int w = 1; w < 8; ++w) Mx = fmaxf(Mx, ml[w].x);
    float L = 0.f, a0 = 0.f, a1 = 0.f;
#pragma unroll
    for (int w = 0; w < 8; ++w) {
      const float wt = __builtin_amdgcn_exp2f(ml[w].x - Mx);
      L = fmaf(wt, ml[w].y, L);
      a0 = fmaf(wt, pv[w].x, a0);
      a1 = fmaf(wt, pv[w].y, a1);
    }
    const float inv = __builtin_amdgcn_rcpf(L);
    ((unsigned*)lds)[L_Q + tid] = cvtpk(a0 * inv, a1 * inv);
  }
  __syncthreads();
  if (tid < 480) {
    float oa[8];
#pragma unroll
    for (int e = 0; e < 8; ++e) oa[e] = 0.f;
    u32x4 op[4];
#pragma unroll
    for (int i = 0; i < 4; ++i) op[i] = *(const u32x4*)&((unsigned*)lds)[L_Q + rg * 16 + i * 4];
#pragma unroll
    for (int mm = 0; mm < 16; ++mm) {
      const unsigned pr = op[mm >> 2][mm & 3];
#pragma unroll
      for (int e = 0; e < 4; ++e) {
        oa[2 * e] = dot2(WO[2 * mm][e], pr, oa[2 * e]);
        oa[2 * e + 1] = dot2(WO[2 * mm + 1][e], pr, oa[2 * e + 1]);
      }
    }
    *(f32x4*)&lds[L_RED + rg * 640 + cc * 8] = f32x4{oa[0], oa[1], oa[2], oa[3]};
    *(f32x4*)&lds[L_RED + rg * 640 + cc * 8 + 4] = f32x4{oa[4], oa[5], oa[6], oa[7]};
  }
  __syncthreads();
  const char* gbase = ws + WS_GRAN + (size_t)b * (8 * NH * 640);
  if (tid < 320) {
    float2 s = *(const float2*)&lds[L_RED + tid * 2];
#pragma unroll
    for (int g = 1; g < 6; ++g) { const float2 t = *(const float2*)&lds[L_RED + g * 640 + tid * 2]; s.x += t.x; s.y += t.y; }
    const u32x4 g4 = {__float_as_uint(s.x), tag, __float_as_uint(s.y), tag};
    GST4_SC1((unsigned)(((tid / 40) * NH + h) * 640 + (tid % 40) * 16), gbase, g4, 0);
  }
  if ((h & 3) == 0 && wave < 5) {
    const char* mine = gbase + (size_t)(h >> 2) * (NH * 640);
    const unsigned mo = r * 640 + wave * 128 + c * 16;
    u32x4 g[4];
    bool ok[4] = {false, false, false, false};
    for (unsigned spins = 0; spins < SPIN_CAP; ++spins) {
#pragma unroll
      for (int k = 0; k < 4; ++k)
        if (!ok[k]) asm volatile("global_load_dwordx4 %0, %1, %2 sc1" : "+v"(g[k]) : "v"(mo + k * 8 * 640), "s"(mine) : "memory");
      asm volatile("s_waitcnt vmcnt(0)" : "+v"(g[0]), "+v"(g[1]), "+v"(g[2]), "+v"(g[3]));
      int missing = 0;
#pragma unroll
      for (int k = 0; k < 4; ++k) { ok[k] = g[k][1] == tag && g[k][3] == tag; missing += __popcll(__ballot(!ok[k])); }
      if (missing == 0) break;
      if (missing > 64) __builtin_amdgcn_s_sleep(PSLOW); else __builtin_amdgcn_s_sleep(PFAST);
    }
    const float s0 = rsum((__uint_as_float(g[0][0]) + __uint_as_float(g[1][0])) + (__uint_as_float(g[2][0]) + __uint_as_float(g[3][0])));
    const float s1 = rsum((__uint_as_float(g[0][2]) + __uint_as_float(g[1][2])) + (__uint_as_float(g[2][2]) + __uint_as_float(g[3][2])));
    if (lane < 8) out[((b * DM + (h >> 2) * 80) >> 1) + wave * 8 + lane] = cvtpk(s0, s1);
    if (tid == 0 && blk == 0) __hip_atomic_store((unsigned*)(ws + WS_EPOCH), tag, __ATOMIC_RELAXED, __HIP_MEMORY_SCOPE_AGENT);
  }
}

extern "C" void kernel_launch(void* const* d_in, const int* in_sizes, int n_in, void* d_out, int out_size, void* d_ws,
                              size_t ws_size, hipStream_t stream) {
  attn_fused<<<256, NTHR, 0, stream>>>((const char*)d_in[0], (const char*)d_in[1], (const char*)d_in[2], (const char*)d_in[3],
                                       (const char*)d_in[4], (unsigned*)d_out, (char*)d_ws);
}
```
